```python
import torch

def _softmax(x, axis=-1):
    return torch.softmax(x, dim=axis)
def _gelu(x):
    return torch.nn.functional.gelu(x, approximate="tanh")
def _silu(x):
    return torch.nn.functional.silu(x)
def _sigmoid(x):
    return torch.sigmoid(x)
def _layernorm(x, axis=-1, eps=1e-5):
    m = x.mean(dim=axis, keepdim=True)
    v = x.var(dim=axis, keepdim=True, unbiased=False)
    return (x - m) / torch.sqrt(v + eps)
def _scatter_add(tgt, idx, src, axis=0):
    return tgt.clone().index_add_(axis, idx, src)
def _segment_sum(src, seg, n):
    out = torch.zeros((n,) + src.shape[1:], dtype=src.dtype)
    return out.index_add_(0, seg, src)
def _pad(x, pw):
    flat = tuple(v for pair in reversed(pw) for v in pair)
    return torch.nn.functional.pad(x, flat)

kernel_name = 'recurrent_scan__gqa_block_cached__mixer_channel_mlp__decode'

def setup_inputs():

    _w = (lambda a: (lambda t: t.to(torch.bfloat16) if t.is_floating_point() else t)(torch.as_tensor(a)))
    def _A_setup():
        B, Sq, D = 2, 1, 1024
        Hq, Hkv, Sk, Dh = 32, 4, 16384, 128
        assert Hq % Hkv == 0, (Hq, Hkv)
        return {
            "x": _w((lambda *s: torch.randn(*s, dtype=torch.bfloat16) * 0.1)(B, Sq, D)),
            "k_cache": _w((lambda *s: torch.randn(*s, dtype=torch.bfloat16) * 0.8)(B, Hkv, Sk, Dh)),
            "v_cache": _w((lambda *s: torch.randn(*s, dtype=torch.bfloat16) * 3.2)(B, Hkv, Sk, Dh)),
            "wq": _w((lambda *s: torch.randn(*s, dtype=torch.bfloat16) * 0.8)(D, Hq * Dh) * (D ** -0.5)),
            "wo": _w((lambda *s: torch.randn(*s, dtype=torch.bfloat16) * 3.2)(Hq * Dh, D) * ((Hq * Dh) ** -0.5)),
        }
    _A_in = _A_setup()

    def _B_setup():
        B, S, D, F = 2, 1, 1024, 3584
        return {
            "x": _w((lambda *s: torch.randn(*s, dtype=torch.bfloat16) * 0.1)(B, S, D)),
            "w1": _w((lambda *s: torch.randn(*s, dtype=torch.bfloat16) * 3.2)(D, F) * (D ** -0.5)),
            "b1": _w((lambda *s: torch.randn(*s, dtype=torch.bfloat16) * 3.2)(F)),
            "w2": _w((lambda *s: torch.randn(*s, dtype=torch.bfloat16) * 3.2)(F, D) * (F ** -0.5)),
            "b2": _w((lambda *s: torch.randn(*s, dtype=torch.bfloat16) * 3.2)(D)),
        }
    _B_in = _B_setup()

    return {
        "A_x": _A_in["x"],
        "A_k_cache": _A_in["k_cache"],
        "A_v_cache": _A_in["v_cache"],
        "A_wq": _A_in["wq"],
        "A_wo": _A_in["wo"],
        "B_x": _B_in["x"],
        "B_w1": _B_in["w1"],
        "B_b1": _B_in["b1"],
        "B_w2": _B_in["w2"],
        "B_b2": _B_in["b2"],
    }

def reference(A_x, A_k_cache, A_v_cache, A_wq, A_wo, B_x, B_w1, B_b1, B_w2, B_b2):
    def _A_ref(x, k_cache, v_cache, wq, wo):
        B, Sq, D = x.shape
        Hq, Dh = 32, 128
        Hkv = k_cache.shape[1]
        rep = Hq // Hkv
        q = (lambda x, *d: x.permute(*d))((x @ wq).reshape(B, Sq, Hq, Dh), 0, 2, 1, 3)
        k = (lambda x, n, axis: torch.repeat_interleave(x, n, dim=axis))(k_cache, rep, axis=1)
        v = (lambda x, n, axis: torch.repeat_interleave(x, n, dim=axis))(v_cache, rep, axis=1)
        scores = torch.einsum("bhqd,bhkd->bhqk", q, k) * (Dh ** -0.5)
        p = _softmax(scores, axis=-1)
        o = torch.einsum("bhqk,bhkd->bhqd", p, v)
        return (lambda x, *d: x.permute(*d))(o, 0, 2, 1, 3).reshape(B, Sq, Hq * Dh) @ wo

    def _B_ref(x, w1, b1, w2, b2):
        y = _layernorm(x, axis=-1)
        h = _gelu(torch.einsum("bsd,df->bsf", y, w1) + b1)
        return torch.einsum("bsf,fd->bsd", h, w2) + b2

    x = A_x
    xn = _layernorm(x, axis=-1)
    s = _A_ref(xn, A_k_cache, A_v_cache, A_wq, A_wo)
    g = _silu(_B_ref(xn, B_w1, B_b1, B_w2, B_b2))
    return x + s * g
```

```cpp
#include <hip/hip_runtime.h>

typedef unsigned u32;
typedef unsigned long long u64;
typedef unsigned short u16;
typedef u32 v4u __attribute__((ext_vector_type(4)));
typedef u32 v2u __attribute__((ext_vector_type(2)));
typedef float v4f __attribute__((ext_vector_type(4)));
typedef __bf16 v8bf __attribute__((ext_vector_type(8)));
typedef __bf16 v2bf __attribute__((ext_vector_type(2)));
#define LAS __attribute__((address_space(3)))
#define GAS __attribute__((address_space(1)))

#define INTERLEAVE 0
#define CODE_PREFETCH 0
#define W2_EARLY 0

namespace {
constexpr int D = 1024, NQ = 4096, DFF = 3584, SK = 16384;
constexpr int NTHR = 768;
constexpr int AUX_NT = 2, AUX_SC1 = 16;
constexpr u32 SENT = 0xFFFFFFFFu;
constexpr float C2 = 0.08838834764831845f * 1.4426950408889634f;
constexpr int WS_EPOCH = 0, WS_FLAGS = 4096, WS_QP = 8192, WS_HP = WS_QP + 4 * 2 * NQ * 4, WS_PA = WS_HP + 4 * 2 * DFF * 4,
              WS_ML = WS_PA + 256 * 1024 * 4, WS_PB = WS_ML + 256 * 64, WS_PG = WS_PB + 64 * 2 * 1024 * 4, WS_END = WS_PG + 64 * 2 * 1024 * 4;
constexpr int L_CTRL = 0, L_XN = 64, L_Y = L_XN + 1024, L_Q = L_Y + 1024, QROW = 272, L_M = L_Q + 8 * QROW, L_L = L_M + 256,
              L_H = L_L + 256, L_O = L_H + 256, OROW = 144, L_ACC = L_O + 16 * OROW, L_END = L_ACC + 8 * 8 * 512;
enum { C_PUB = 0, C_S1, C_Q, C_H, C_VISS, C_MX, C_LS, C_ACC, C_O };
constexpr u32 LDS_SPIN_CAP = 1u << 18, G_SPIN_CAP = 1u << 14;

#define NSTAMP 16
#define STAMP_OFF (128ull << 20)
#define ST(i)
#define ST_FLUSH()

struct Params {
    const u16 *x, *k, *v, *wq, *wo, *w1, *b1, *w2, *b2;
    u16* out;
    unsigned char* ws;
};

typedef __amdgpu_buffer_rsrc_t rsrc_t;
__device__ __forceinline__ rsrc_t mk_rsrc(const void* p, int bytes) { return __builtin_amdgcn_make_buffer_rsrc((void*)p, 0, bytes, 0x00020000); }
template <int AUX> __device__ __forceinline__ v4u ld16(rsrc_t r, int voff, int soff) { return __builtin_amdgcn_raw_buffer_load_b128(r, voff, soff, AUX); }
template <int AUX> __device__ __forceinline__ v2u ld8(rsrc_t r, int voff, int soff) { return __builtin_amdgcn_raw_buffer_load_b64(r, voff, soff, AUX); }
__device__ __forceinline__ void st16(v4u d, rsrc_t r, int voff, int soff) { __builtin_amdgcn_raw_buffer_store_b128(d, r, voff, soff, AUX_SC1); }
__device__ __forceinline__ void st8(v2u d, rsrc_t r, int voff, int soff) { __builtin_amdgcn_raw_buffer_store_b64(d, r, voff, soff, AUX_SC1); }
#define FENCE() do { asm volatile("" ::: "memory"); __builtin_amdgcn_sched_barrier(0); } while (0)
#define DRAIN_VM() asm volatile("s_waitcnt vmcnt(0)" ::: "memory")
#define DRAIN_LDS() asm volatile("s_waitcnt lgkmcnt(0)" ::: "memory")

__device__ __forceinline__ float u2f(u32 u) { return __builtin_bit_cast(float, u); }
__device__ __forceinline__ u32 f2u(float f) { return __builtin_bit_cast(u32, f); }
__device__ __forceinline__ float blo(u32 u) { return u2f(u << 16); }
__device__ __forceinline__ float bhi(u32 u) { return u2f(u & 0xFFFF0000u); }
__device__ __forceinline__ u32 pk(float a, float b) { v2bf r = {(__bf16)a, (__bf16)b}; return __builtin_bit_cast(u32, r); }
__device__ __forceinline__ float rbf(float a) { return (float)(__bf16)a; }
__device__ __forceinline__ u32 nosent(float f) { u32 u = f2u(f); return u < 0xFFFFFFFEu ? u : 0xFFFFFFFEu; }
__device__ __forceinline__ bool has_sent(v4u a) { return a.x == SENT || a.y == SENT || a.z == SENT || a.w == SENT; }
template <int CTRL> __device__ __forceinline__ float dpp(float x) { return u2f(__builtin_amdgcn_mov_dpp(f2u(x), CTRL, 0xf, 0xf, true)); }
constexpr int XOR1 = 0xB1, XOR2 = 0x4E, XOR7 = 0x141, ROR8 = 0x128;
__device__ __forceinline__ float tr32(float a, float b) { auto s = __builtin_amdgcn_permlane32_swap(f2u(a), f2u(b), false, false); return u2f(s[0]) + u2f(s[1]); }
__device__ __forceinline__ float tr16(float a, float b) { auto s = __builtin_amdgcn_permlane16_swap(f2u(a), f2u(b), false, false); return u2f(s[0]) + u2f(s[1]); }
__device__ __forceinline__ float tr8(float a, float b, bool bit) { const float own = bit ? b : a, oth = bit ? a : b; return own + dpp<ROR8>(oth); }
__device__ __forceinline__ float max32(float a) { auto s = __builtin_amdgcn_permlane32_swap(f2u(a), f2u(a), false, false); return fmaxf(u2f(s[0]), u2f(s[1])); }
__device__ __forceinline__ float max16(float a) { auto s = __builtin_amdgcn_permlane16_swap(f2u(a), f2u(a), false, false); return fmaxf(u2f(s[0]), u2f(s[1])); }
__device__ __forceinline__ float sum8lo(float a) { a += dpp<XOR1>(a); a += dpp<XOR2>(a); a += dpp<XOR7>(a); return a; }
__device__ __forceinline__ float sum32lo(float a) { a = sum8lo(a); a += dpp<ROR8>(a); return tr16(a, a); }
__device__ __forceinline__ float max32lo(float a) { a = fmaxf(a, dpp<XOR1>(a)); a = fmaxf(a, dpp<XOR2>(a)); a = fmaxf(a, dpp<XOR7>(a)); a = fmaxf(a, dpp<ROR8>(a)); return max16(a); }
__device__ __forceinline__ float sum64(float a) { a = sum32lo(a); return tr32(a, a); }
__device__ __forceinline__ float dot2(u32 a, u32 b, float c) { return __builtin_amdgcn_fdot2_f32_bf16(__builtin_bit_cast(v2bf, a), __builtin_bit_cast(v2bf, b), c, false); }
__device__ __forceinline__ v4f mfma(v4u a, v4u b, v4f c) { return __builtin_amdgcn_mfma_f32_16x16x32_bf16(__builtin_bit_cast(v8bf, a), __builtin_bit_cast(v8bf, b), c, 0, 0, 0); }
__device__ __forceinline__ u32 plo(u32 r0, u32 r1) { return __builtin_amdgcn_perm(r1, r0, 0x05040100u); }
__device__ __forceinline__ u32 phi(u32 r0, u32 r1) { return __builtin_amdgcn_perm(r1, r0, 0x07060302u); }
template <int DD> __device__ __forceinline__ v4u col_frag(const v4u* r) {
    constexpr int w = DD >> 1;
    if constexpr (DD & 1) return v4u{phi(r[0][w], r[1][w]), phi(r[2][w], r[3][w]), phi(r[4][w], r[5][w]), phi(r[6][w], r[7][w])};
    else return v4u{plo(r[0][w], r[1][w]), plo(r[2][w], r[3][w]), plo(r[4][w], r[5][w]), plo(r[6][w], r[7][w])};
}
__device__ __forceinline__ void rows_mfma(v4u a, const v4u* r, v4f* acc) {
    acc[0] = mfma(a, col_frag<0>(r), acc[0]); acc[1] = mfma(a, col_frag<1>(r), acc[1]);
    acc[2] = mfma(a, col_frag<2>(r), acc[2]); acc[3] = mfma(a, col_frag<3>(r), acc[3]);
    acc[4] = mfma(a, col_frag<4>(r), acc[4]); acc[5] = mfma(a, col_frag<5>(r), acc[5]);
    acc[6] = mfma(a, col_frag<6>(r), acc[6]); acc[7] = mfma(a, col_frag<7>(r), acc[7]);
}
__device__ __forceinline__ u64 mix64(u64 z) { z += 0x9E3779B97F4A7C15ull; z = (z ^ (z >> 30)) * 0xBF58476D1CE4E5B9ull; z = (z ^ (z >> 27)) * 0x94D049BB133111EBull; return z ^ (z >> 31); }

__device__ __forceinline__ void lds_wait(LAS u32* ctrl, int idx, u32 want) {
#pragma unroll 1
    for (u32 s = 0; s < LDS_SPIN_CAP; ++s) {
        if ((u32)__builtin_amdgcn_readfirstlane(*(volatile LAS u32*)(ctrl + idx)) >= want) break;
        __builtin_amdgcn_s_sleep(1);
    }
    FENCE();
}
__device__ __forceinline__ u32 lds_arrive(LAS u32* ctrl, int idx, int lane) {
    DRAIN_LDS();
    u32 old = 0;
    if (lane == 0) old = __hip_atomic_fetch_add(ctrl + idx, 1u, __ATOMIC_RELAXED, __HIP_MEMORY_SCOPE_WORKGROUP);
    old = __builtin_amdgcn_readfirstlane(old);
    FENCE();
    return old;
}

__device__ __forceinline__ void layernorm16(const float (&v)[16], float (&o)[16]) {
    float s = 0.f;
#pragma unroll
    for (int i = 0; i < 16; ++i) s += v[i];
    const float mean = sum64(s) * (1.0f / D);
    float q = 0.f;
#pragma unroll
    for (int i = 0; i < 16; ++i) { const float d = v[i] - mean; q = fmaf(d, d, q); }
    const float var = rbf(sum64(q) * (1.0f / D)), m = rbf(mean);
    const float den = rbf(__builtin_sqrtf(rbf(var + 1e-5f))), inv = 1.0f / den;
#pragma unroll
    for (int i = 0; i < 16; ++i) o[i] = rbf(rbf(v[i] - m) * inv);
}

__device__ __forceinline__ float gather_cols(rsrc_t ws, int off, int lane) {
    v4u a, b;
#pragma unroll 1
    for (u32 s = 0; s < G_SPIN_CAP; ++s) {
        a = ld16<AUX_SC1>(ws, off, 0); b = ld16<AUX_SC1>(ws, off + 16, 0);
        if (!__any(has_sent(a) || has_sent(b))) break;
        FENCE();
    }
    const float t0 = tr32(u2f(a.x), u2f(b.x)), t1 = tr32(u2f(a.y), u2f(b.y)), t2 = tr32(u2f(a.z), u2f(b.z)), t3 = tr32(u2f(a.w), u2f(b.w));
    return sum8lo(tr8(tr16(t0, t2), tr16(t1, t3), lane & 8));
}
}

__global__ void __launch_bounds__(NTHR, 3) fused_decode(Params p) {
    __shared__ __attribute__((aligned(16))) unsigned char smem[L_END];
    LAS unsigned char* L = (LAS unsigned char*)smem;
    LAS u32* ctrl = (LAS u32*)(L + L_CTRL);
    const int c = blockIdx.x, lane = threadIdx.x & 63, wave = __builtin_amdgcn_readfirstlane(threadIdx.x >> 6);
    const int g = lane >> 4, c16 = lane & 15;
    const rsrc_t ws = mk_rsrc(p.ws, WS_END);
    const int rb = c >> 2, cb4 = c & 3;
    ST(0);
    if (wave < 2) {
        __builtin_amdgcn_s_setprio(3);
        if (wave == 0 && lane < 16) ctrl[lane] = 0;
        const int kq = c & 3, cb = c >> 2;
        const v2u ep = ld8<AUX_SC1>(ws, WS_EPOCH, 0);
        const rsrc_t rx = mk_rsrc(p.x, 2 * D * 2);
        v4u xr[2][2];
#pragma unroll
        for (int b = 0; b < 2; ++b) { xr[b][0] = ld16<0>(rx, lane * 32, b * 2048); xr[b][1] = ld16<0>(rx, lane * 32 + 16, b * 2048); }
        const int rowb = wave == 0 ? NQ * 2 : DFF * 2;
        const rsrc_t rw = mk_rsrc(wave == 0 ? p.wq : p.w1, wave == 0 ? D * NQ * 2 : (cb < 56 ? D * DFF * 2 : 0));
        const int woff = (256 * kq + 32 * (lane >> 3)) * rowb + cb * 128 + (lane & 7) * 16;
        v4u W[32];
#pragma unroll
        for (int i = 0; i < 32; ++i) W[i] = ld16<AUX_NT>(rw, woff, i * rowb);
        FENCE();
        asm volatile("s_waitcnt lgkmcnt(0)\n\ts_barrier" ::: "memory");
        FENCE();
        ST(1);
        LAS unsigned char* act = L + (wave == 0 ? L_XN : L_Y);
#pragma unroll
        for (int b = 0; b < 2; ++b) {
            float v[16], o[16];
#pragma unroll
            for (int i = 0; i < 8; ++i) { const u32 u = xr[b][i >> 2][i & 3]; v[2 * i] = blo(u); v[2 * i + 1] = bhi(u); }
            layernorm16(v, o);
            if (wave == 1) { layernorm16(o, v);
#pragma unroll
                for (int i = 0; i < 16; ++i) o[i] = v[i]; }
            if (g == kq) {
                *(LAS v4u*)(act + b * 512 + c16 * 32) = v4u{pk(o[0], o[1]), pk(o[2], o[3]), pk(o[4], o[5]), pk(o[6], o[7])};
                *(LAS v4u*)(act + b * 512 + c16 * 32 + 16) = v4u{pk(o[8], o[9]), pk(o[10], o[11]), pk(o[12], o[13]), pk(o[14], o[15])};
            }
        }
        FENCE();
        ST(2);
        float acc[2][8];
#pragma unroll
        for (int e = 0; e < 8; ++e) acc[0][e] = acc[1][e] = 0.f;
#pragma unroll
        for (int m4 = 0; m4 < 4; ++m4) {
            const v4u a0 = *(const LAS v4u*)(act + (lane >> 3) * 64 + m4 * 16), a1 = *(const LAS v4u*)(act + 512 + (lane >> 3) * 64 + m4 * 16);
#pragma unroll
            for (int mm = 0; mm < 4; ++mm) {
                const v4u r0 = W[8 * m4 + 2 * mm], r1 = W[8 * m4 + 2 * mm + 1];
#pragma unroll
                for (int w = 0; w < 4; ++w) {
                    const u32 lo = plo(r0[w], r1[w]), hi = phi(r0[w], r1[w]);
                    acc[0][2 * w] = dot2(lo, a0[mm], acc[0][2 * w]); acc[1][2 * w] = dot2(lo, a1[mm], acc[1][2 * w]);
                    acc[0][2 * w + 1] = dot2(hi, a0[mm], acc[0][2 * w + 1]); acc[1][2 * w + 1] = dot2(hi, a1[mm], acc[1][2 * w + 1]);
                }
            }
        }
        {
            float r[8], t[4];
#pragma unroll
            for (int e = 0; e < 8; ++e) r[e] = tr32(acc[0][e], acc[1][e]);
#pragma unroll
            for (int e = 0; e < 4; ++e) t[e] = tr16(r[e], r[e + 4]);
            const float u0 = tr8(t[0], t[2], lane & 8), u1 = tr8(t[1], t[3], lane & 8);
            const int col = cb * 64 + 8 * (lane & 7) + 4 * (g & 1) + 2 * ((lane >> 3) & 1), b = lane >> 5;
            const int off = wave == 0 ? WS_QP + ((kq * 2 + b) * NQ + col) * 4 : (cb < 56 ? WS_HP + ((kq * 2 + b) * DFF + col) * 4 : WS_END);
            st8(v2u{f2u(u0), f2u(u1)}, ws, off, 0);
        }
        ST(3);
        DRAIN_VM();
        lds_arrive(ctrl, C_PUB, lane);
        ST(4);
        if (wave == 0) {
            const u64 tag = mix64(((u64)ep.y << 32) | ep.x);
            const u32 tl = (u32)tag, th = (u32)(tag >> 32);
            lds_wait(ctrl, C_PUB, 4);
            if (lane == 0) st8(v2u{tl, th}, ws, WS_FLAGS + c * 8, 0);
#pragma unroll 1
            for (u32 s = 0; s < G_SPIN_CAP; ++s) {
                const v4u f0 = ld16<AUX_SC1>(ws, WS_FLAGS + lane * 16, 0), f1 = ld16<AUX_SC1>(ws, WS_FLAGS + 1024 + lane * 16, 0);
                const bool ok = f0.x == tl && f0.y == th && f0.z == tl && f0.w == th && f1.x == tl && f1.y == th && f1.z == tl && f1.w == th;
                if (__all(ok)) break;
                __builtin_amdgcn_s_sleep(8);
                FENCE();
            }
            FENCE();
            lds_arrive(ctrl, C_S1, lane);
            ST(5);
            if (c == 0 && lane == 0) st8(v2u{tl, th}, ws, WS_EPOCH, 0);
            v4u qp[4][4];
            const int qo = WS_QP + ((c >> 7) * NQ + ((c >> 5) & 3) * 1024 + lane * 4) * 4;
#pragma unroll
            for (int i = 0; i < 4; ++i)
#pragma unroll
                for (int k4 = 0; k4 < 4; ++k4) qp[i][k4] = ld16<AUX_SC1>(ws, qo, k4 * 2 * NQ * 4 + i * 1024);
            FENCE();
#pragma unroll
            for (int i = 0; i < 4; ++i) {
                float s4[4];
#pragma unroll
                for (int e = 0; e < 4; ++e) s4[e] = (u2f(qp[i][0][e]) + u2f(qp[i][1][e])) + (u2f(qp[i][2][e]) + u2f(qp[i][3][e]));
                *(LAS v2u*)(L + L_Q + (2 * i + (lane >> 5)) * QROW + (lane & 31) * 8) = v2u{pk(s4[0], s4[1]), pk(s4[2], s4[3])};
            }
            lds_arrive(ctrl, C_Q, lane);
            ST(6);
        } else {
            const rsrc_t rb1 = mk_rsrc(p.b1, DFF * 2);
            const int hb = (lane >> 4) & 1, col = 56 * rb + 4 * (c16 < 14 ? c16 : 0);
            const v2u bias = ld8<0>(rb1, col * 2, 0);
            lds_wait(ctrl, C_S1, 1);
            ST(5);
            v4u hp[4];
#pragma unroll
            for (int k4 = 0; k4 < 4; ++k4) hp[k4] = ld16<AUX_SC1>(ws, WS_HP + (hb * DFF + col) * 4, k4 * 2 * DFF * 4);
            FENCE();
#pragma unroll
            for (int e = 0; e < 4; ++e) {
                const float s = (u2f(hp[0][e]) + u2f(hp[1][e])) + (u2f(hp[2][e]) + u2f(hp[3][e]));
                const float u = rbf(rbf(s) + ((e & 1) ? bhi(bias[e >> 1]) : blo(bias[e >> 1])));
                const float z2 = 2.0f * 0.7978845608028654f * 1.4426950408889634f * fmaf(0.044715f * u * u, u, u);
                const float h = u / (1.0f + __builtin_amdgcn_exp2f(-z2));
                if (lane < 32) *(LAS u16*)(L + L_H + ((hb * 2 + (c16 >> 3)) * 4 + e) * 16 + (c16 & 7) * 2) = c16 < 14 ? (u16)pk(h, 0.f) : (u16)0;
            }
            lds_arrive(ctrl, C_H, lane);
            ST(6);
        }
        if (!W2_EARLY) lds_wait(ctrl, C_VISS, 8);
        const rsrc_t rw2 = mk_rsrc(p.w2, DFF * D * 2);
        v4u R[16];
#pragma unroll
        for (int i = 0; i < 14; ++i) R[i] = ld16<AUX_NT>(rw2, (56 * rb + g) * 2048 + cb4 * 512 + wave * 256 + c16 * 16, i * 8192);
        R[14] = R[15] = v4u{0, 0, 0, 0};
        FENCE();
        ST(7);
        lds_wait(ctrl, C_H, 1);
        {
            const v4u a0 = *(const LAS v4u*)(L + L_H + ((lane & 1) * 8 + g) * 16), a1 = *(const LAS v4u*)(L + L_H + ((lane & 1) * 8 + 4 + g) * 16);
            v4f ac[8];
#pragma unroll
            for (int e = 0; e < 8; ++e) ac[e] = v4f{0.f, 0.f, 0.f, 0.f};
            rows_mfma(a0, R, ac);
            rows_mfma(a1, R + 8, ac);
            v4u o;
#pragma unroll
            for (int e = 0; e < 4; ++e) {
                const float l0 = ac[e][0], l1 = ac[e][1], h0 = ac[4 + e][0], h1 = ac[4 + e][1];
                const float t0 = (g & 1) ? h0 : l0, t1 = (g & 1) ? h1 : l1;
                o[e] = nosent((g & 2) ? t1 : t0);
            }
            st16(o, ws, WS_PG + ((rb * 2 + (g >> 1)) * 1024 + cb4 * 256 + wave * 128 + c16 * 8 + (g & 1) * 4) * 4, 0);
        }
        ST(8);
        if (wave == 0) {
            const int e = 4 * (lane >> 5) + 2 * (g & 1) + ((lane >> 3) & 1), n = (c & 127) * 8 + e;
            const float xv = u2f((u32)p.x[(c >> 7) * D + n] << 16), b2v = u2f((u32)p.b2[n] << 16);
            const int off = ((lane * 2 + (c >> 7)) * 1024 + (c & 127) * 8) * 4;
            const float u = rbf(rbf(gather_cols(ws, WS_PG + off, lane)) + b2v);
            const float gate = rbf(u / (1.0f + __builtin_amdgcn_exp2f(-1.4426950408889634f * u)));
            ST(9);
            lds_wait(ctrl, C_O, 8);
            ST(10);
            const float s = rbf(gather_cols(ws, WS_PB + off, lane));
            ST(11);
            if ((lane & 7) == 0) p.out[(c >> 7) * D + n] = (u16)pk(xv + rbf(s * gate), 0.f);
        }
        ST(12);
    } else if (wave < 4) {
        __builtin_amdgcn_s_setprio(2);
        const v4u sent = {SENT, SENT, SENT, SENT};
        if (wave == 2) {
#pragma unroll
            for (int i = 0; i < 4; ++i) st16(sent, ws, WS_PA + c * 4096 + lane * 16, i * 1024);
            st16(sent, ws, lane < 4 ? WS_ML + c * 64 + lane * 16 : WS_END, 0);
        } else {
#pragma unroll
            for (int i = 0; i < 4; ++i) st16(sent, ws, WS_PB + (rb * 2 * 1024 + cb4 * 256) * 4 + lane * 16, (i & 1) * 4096 + (i >> 1) * (WS_PG - WS_PB));
        }
        FENCE();
        asm volatile("s_barrier" ::: "memory");
        FENCE();
        DRAIN_VM();
        lds_arrive(ctrl, C_PUB, lane);
        ST(1);
        lds_wait(ctrl, C_VISS, 8);
        const rsrc_t rwo = mk_rsrc(p.wo, NQ * D * 2);
        v4u R[16];
#pragma unroll
        for (int i = 0; i < 16; ++i) R[i] = ld16<AUX_NT>(rwo, (64 * rb + g) * 2048 + cb4 * 512 + (wave - 2) * 256 + c16 * 16, i * 8192);
        FENCE();
        ST(7);
        v4u T[16];
        T[0] = col_frag<0>(R); T[1] = col_frag<1>(R); T[2] = col_frag<2>(R); T[3] = col_frag<3>(R);
        T[4] = col_frag<4>(R); T[5] = col_frag<5>(R); T[6] = col_frag<6>(R); T[7] = col_frag<7>(R);
        T[8] = col_frag<0>(R + 8); T[9] = col_frag<1>(R + 8); T[10] = col_frag<2>(R + 8); T[11] = col_frag<3>(R + 8);
        T[12] = col_frag<4>(R + 8); T[13] = col_frag<5>(R + 8); T[14] = col_frag<6>(R + 8); T[15] = col_frag<7>(R + 8);
#pragma unroll
        for (int i = 0; i < 16; ++i) asm volatile("" : "+v"(T[i]));
        ST(8);
        lds_wait(ctrl, C_O, 8);
        ST(9);
        const v4u a0 = *(const LAS v4u*)(L + L_O + c16 * OROW + g * 16), a1 = *(const LAS v4u*)(L + L_O + c16 * OROW + 64 + g * 16);
        float t[8];
#pragma unroll
        for (int e = 0; e < 8; ++e) {
            v4f ac = mfma(a0, T[e], v4f{0.f, 0.f, 0.f, 0.f});
            ac = mfma(a1, T[8 + e], ac);
            t[e] = (ac[0] + ac[1]) + (ac[2] + ac[3]);
        }
        v4u o;
#pragma unroll
        for (int e = 0; e < 4; ++e) o[e] = nosent(tr32(t[e], t[e + 4]));
        st16(o, ws, WS_PB + ((rb * 2 + (g & 1)) * 1024 + cb4 * 256 + (wave - 2) * 128 + c16 * 8 + (g >> 1) * 4) * 4, 0);
        ST(12);
    } else {
        const int kw = wave - 4;
        asm volatile("s_barrier" ::: "memory");
        FENCE();
        ST(1);
        const rsrc_t rk = mk_rsrc(p.k, 2 * 4 * SK * 128 * 2), rv = mk_rsrc(p.v, 2 * 4 * SK * 128 * 2);
        const int tb = INTERLEAVE ? (c >> 5) * (SK * 256) + ((c & 31) * 8 + kw) * 4096 : c * 131072 + kw * 16384;
        constexpr int TS = INTERLEAVE ? 256 * 4096 : 4096;
        v4u K[16], V0[8], V1[8];
        {
            const int ko = tb + (lane & 7) * 256 + (g + 4 * ((lane >> 3) & 1)) * 16;
#pragma unroll
            for (int i = 0; i < 16; ++i) K[i] = ld16<AUX_NT>(rk, ko + (i & 1) * 2048 + ((i >> 1) & 1) * 128, (i >> 2) * TS);
        }
        const int vo = tb + g * 1024 + c16 * 16;
#pragma unroll
        for (int j = 0; j < 8; ++j) V0[j] = ld16<AUX_NT>(rv, vo + (j & 3) * 256, (j >> 2) * TS);
        FENCE();
        ST(2);
#pragma unroll
        for (int i = 0; i < 16; i += 2)
#pragma unroll
            for (int w = 0; w < 4; ++w) {
                const u32 a = K[i][w], b = K[i + 1][w];
                K[i][w] = __builtin_amdgcn_update_dpp(a, b, ROR8, 0xf, 0xc, false);
                K[i + 1][w] = __builtin_amdgcn_update_dpp(b, a, ROR8, 0xf, 0x3, false);
            }
#pragma unroll
        for (int i = 0; i < 16; ++i) asm volatile("" : "+v"(K[i]));
        ST(3);
        lds_wait(ctrl, C_Q, 1);
        ST(4);
        v4f S[4];
        {
            v4u q[4];
#pragma unroll
            for (int cc = 0; cc < 4; ++cc) q[cc] = *(const LAS v4u*)(L + L_Q + (lane & 7) * QROW + cc * 64 + g * 16);
#pragma unroll
            for (int t = 0; t < 4; ++t) {
                S[t] = v4f{0.f, 0.f, 0.f, 0.f};
#pragma unroll
                for (int cc = 0; cc < 4; ++cc) S[t] = mfma(K[4 * t + cc], q[cc], S[t]);
            }
        }
        FENCE();
#pragma unroll
        for (int j = 0; j < 8; ++j) V1[j] = ld16<AUX_NT>(rv, vo + (j & 3) * 256, (2 + (j >> 2)) * TS);
        FENCE();
        lds_arrive(ctrl, C_VISS, lane);
        ST(5);
        float m = S[0][0];
#pragma unroll
        for (int i = 1; i < 16; ++i) m = fmaxf(m, S[i >> 2][i & 3]);
        m = max32(max16(m));
        if (lane < 8) *(LAS float*)(L + L_M + lane * 32 + kw * 4) = m;
        lds_arrive(ctrl, C_MX, lane);
        lds_wait(ctrl, C_MX, 8);
        {
            const v4f m0 = *(const LAS v4f*)(L + L_M + (lane & 7) * 32), m1 = *(const LAS v4f*)(L + L_M + (lane & 7) * 32 + 16);
            m = fmaxf(fmaxf(fmaxf(m0[0], m0[1]), fmaxf(m0[2], m0[3])), fmaxf(fmaxf(m1[0], m1[1]), fmaxf(m1[2], m1[3]))) * C2;
        }
        float l = 0.f;
#pragma unroll
        for (int i = 0; i < 16; ++i) { const float e = __builtin_amdgcn_exp2f(fmaf(S[i >> 2][i & 3], C2, -m)); S[i >> 2][i & 3] = e; l += e; }
        const v4u P0 = {pk(S[0][0], S[0][1]), pk(S[0][2], S[0][3]), pk(S[1][0], S[1][1]), pk(S[1][2], S[1][3])};
        const v4u P1 = {pk(S[2][0], S[2][1]), pk(S[2][2], S[2][3]), pk(S[3][0], S[3][1]), pk(S[3][2], S[3][3])};
        l = tr16(l, l); l = tr32(l, l);
        if (lane < 8) *(LAS float*)(L + L_L + lane * 32 + kw * 4) = l;
        if (lds_arrive(ctrl, C_LS, lane) == 7) {
            const v4f l0 = *(const LAS v4f*)(L + L_L + (lane & 7) * 32), l1 = *(const LAS v4f*)(L + L_L + (lane & 7) * 32 + 16);
            const float lt = ((l0[0] + l0[1]) + (l0[2] + l0[3])) + ((l1[0] + l1[1]) + (l1[2] + l1[3]));
            st8(v2u{nosent(m), nosent(lt)}, ws, lane < 8 ? WS_ML + c * 64 + lane * 8 : WS_END, 0);
        }
        ST(6);
        v4f acc[8];
#pragma unroll
        for (int e = 0; e < 8; ++e) acc[e] = v4f{0.f, 0.f, 0.f, 0.f};
        rows_mfma(P0, V0, acc);
        FENCE();
        ST(7);
        rows_mfma(P1, V1, acc);
#pragma unroll
        for (int r = 0; r < 4; ++r) {
            v4f t;
#pragma unroll
            for (int e = 0; e < 4; ++e) { const float lo = acc[e][r], hi = acc[4 + e][r]; t[e] = (g & 2) ? hi : lo; }
            *(LAS v4f*)(L + L_ACC + kw * 4096 + (4 * (g & 1) + r) * 512 + (g >> 1) * 256 + c16 * 16) = t;
        }
        ST(8);
        lds_arrive(ctrl, C_ACC, lane);
        lds_wait(ctrl, C_ACC, 8);
        {
            v4f t = {0.f, 0.f, 0.f, 0.f};
#pragma unroll
            for (int w = 0; w < 4; ++w) t += *(const LAS v4f*)(L + L_ACC + (4 * (lane >> 5) + w) * 4096 + kw * 512 + (lane & 31) * 16);
            v4u o;
#pragma unroll
            for (int e = 0; e < 4; ++e) o[e] = nosent(tr32(t[e], t[e]));
            st16(o, ws, lane < 32 ? WS_PA + c * 4096 + kw * 512 + c16 * 32 + (g & 1) * 16 : WS_END, 0);
        }
        ST(9);
        {
            const int pc = ((kw >> 2) * 4 + (rb >> 4)) * 32, hh = (rb >> 1) & 7;
            const int s0 = (8 * kw + g) & 31;
            const int ao = WS_PA + (pc + s0) * 4096 + hh * 512 + (rb & 1) * 256 + c16 * 16;
            v2u ml = ld8<AUX_SC1>(ws, WS_ML + (pc + (lane & 31)) * 64 + hh * 8, 0);
            v4u a0 = ld16<AUX_SC1>(ws, ao, 0), a1 = ld16<AUX_SC1>(ws, ao, 4 * 4096);
            FENCE();
#pragma unroll 1
            for (u32 s = 0; s < G_SPIN_CAP; ++s) {
                if (!__any(ml.x == SENT || ml.y == SENT)) break;
                FENCE();
                ml = ld8<AUX_SC1>(ws, WS_ML + (pc + (lane & 31)) * 64 + hh * 8, 0);
            }
            const float ms = u2f(ml.x), wgt = __builtin_amdgcn_exp2f(ms - max32lo(ms));
            const float sc = wgt / sum32lo(wgt * u2f(ml.y));
            const float sc0 = u2f(__builtin_amdgcn_ds_bpermute(s0 * 4, f2u(sc))), sc1 = u2f(__builtin_amdgcn_ds_bpermute((s0 + 4) * 4, f2u(sc)));
#pragma unroll 1
            for (u32 s = 0; s < G_SPIN_CAP; ++s) {
                if (!__any(has_sent(a0) || has_sent(a1))) break;
                FENCE();
                a0 = ld16<AUX_SC1>(ws, ao, 0); a1 = ld16<AUX_SC1>(ws, ao, 4 * 4096);
            }
            ST(10);
            float t[4];
#pragma unroll
            for (int e = 0; e < 4; ++e) t[e] = fmaf(sc0, u2f(a0[e]), sc1 * u2f(a1[e]));
            const float o = tr16(tr32(t[0], t[2]), tr32(t[1], t[3]));
            const float hi = rbf(o);
            const int oo = L_O + kw * OROW + (c16 >> 3) * 64 + g * 16 + (c16 & 7) * 2;
            *(LAS u16*)(L + oo) = (u16)pk(hi, 0.f);
            *(LAS u16*)(L + oo + 8 * OROW) = (u16)pk(o - hi, 0.f);
            lds_arrive(ctrl, C_O, lane);
        }
        ST(12);
    }
    ST_FLUSH();
}

extern "C" void kernel_launch(void* const* d_in, const int* in_sizes, int n_in, void* d_out, int out_size, void* d_ws, size_t ws_size,
                              hipStream_t stream) {
    Params p;
    p.x = (const u16*)d_in[0]; p.k = (const u16*)d_in[1]; p.v = (const u16*)d_in[2]; p.wq = (const u16*)d_in[3]; p.wo = (const u16*)d_in[4];
    p.w1 = (const u16*)d_in[6]; p.b1 = (const u16*)d_in[7]; p.w2 = (const u16*)d_in[8]; p.b2 = (const u16*)d_in[9];
    p.out = (u16*)d_out; p.ws = (unsigned char*)d_ws;
    fused_decode<<<256, NTHR, 0, stream>>>(p);
}
```

```cpp
#include <hip/hip_runtime.h>
#include <type_traits>

typedef unsigned u32;
typedef unsigned long long u64;
typedef unsigned short u16;
typedef u32 v4u __attribute__((ext_vector_type(4)));
typedef u32 v2u __attribute__((ext_vector_type(2)));
typedef float v4f __attribute__((ext_vector_type(4)));
typedef float v2f __attribute__((ext_vector_type(2)));
typedef __bf16 v8bf __attribute__((ext_vector_type(8)));
typedef __bf16 v2bf __attribute__((ext_vector_type(2)));
#define LAS __attribute__((address_space(3)))
#define GAS __attribute__((address_space(1)))

#define CODE_PREFETCH 14336
#define KWIN 2
#define KLATE 7
#define T0 15
#define DT 31
#define WWIN 4
#define WLATE 8
#define T2 1025
#define DT2 13
#define TREAD 270
#define TQ 500
#define QKPOS 10
#define TA 1250
#define ASLACK 10
#define AMAX 300
#define HPOS 4
#define TH 1315
#define TBT 1550
#define BMIN 42
#define BMAX 90

namespace {
constexpr int D = 1024, NQ = 4096, DFF = 3584, SK = 16384;
constexpr int NTHR = 768;
constexpr int AUX_NT = 2, AUX_SC1 = 16;
constexpr float C2 = 0.08838834764831845f * 1.4426950408889634f;
constexpr int WS_EPOCH = 0, WS_T0 = 1024, WS_QP = 4096, WS_HP = WS_QP + 4 * 2 * NQ * 4, WS_PA = WS_HP + 4 * 2 * DFF * 8,
              WS_ML = WS_PA + 256 * 4096, WS_PB = WS_ML + 256 * 128, WS_PG = WS_PB + 64 * 2 * 1024 * 4, WS_END = WS_PG + 64 * 2 * 1024 * 4;
constexpr int WS_OOB = WS_END;
constexpr int L_CTRL = 0, L_ZERO = 64, L_ONE = L_ZERO + 80, L_OM = 192, L_XN = 256, L_Y = L_XN + 4096, L_X2 = L_Y + 4096, L_Q = L_X2 + 2048, QROW = 272, L_M = L_Q + 8 * QROW, L_L = L_M + 256,
              L_H = L_L + 256, L_O = L_H + 256, OROW = 144, L_ACC = L_O + 16 * OROW, L_END = L_ACC + 8 * 8 * 512;
enum { C_XN = 0, C_Y, C_PQ, C_PH, C_Q, C_H, C_VISS, C_MX, C_ACC, C_O, C_LATE, C_PB, C_TAG = 14, C_BASE = 40 };
constexpr u32 LDS_SPIN_CAP = 1u << 22, G_SPIN_CAP = 1u << 19;

struct Params {
    const u16 *x, *wq, *w1;
    unsigned char* ws;
    const u16 *k, *v, *wo, *w2, *b1, *b2;
    u16* out;
};

static_assert(sizeof(Params) == 88, "the kernel entry reads the argument block by offset");

typedef __amdgpu_buffer_rsrc_t rsrc_t;
__device__ __forceinline__ rsrc_t mk_rsrc(const void* p, int bytes) { return __builtin_amdgcn_make_buffer_rsrc((void*)p, 0, bytes, 0x00020000); }
template <int AUX> __device__ __forceinline__ v4u ld16(rsrc_t r, int voff, int soff) { return __builtin_amdgcn_raw_buffer_load_b128(r, voff, soff, AUX); }
template <int AUX> __device__ __forceinline__ v2u ld8(rsrc_t r, int voff, int soff) { return __builtin_amdgcn_raw_buffer_load_b64(r, voff, soff, AUX); }
__device__ __forceinline__ void st16(v4u d, rsrc_t r, int voff, int soff) { __builtin_amdgcn_raw_buffer_store_b128(d, r, voff, soff, AUX_SC1); }
template <int AUX> __device__ __forceinline__ u32 ld4(rsrc_t r, int voff, int soff) { return __builtin_amdgcn_raw_buffer_load_b32(r, voff, soff, AUX); }
__device__ __forceinline__ void st8(v2u d, rsrc_t r, int voff, int soff) { __builtin_amdgcn_raw_buffer_store_b64(d, r, voff, soff, AUX_SC1); }
#define FENCE() do { asm volatile("" ::: "memory"); __builtin_amdgcn_sched_barrier(0); } while (0)
#define PACE(n) do { __builtin_amdgcn_sched_barrier(0); asm volatile("s_waitcnt vmcnt(%0)" :: "n"((n) - 1) : "memory"); __builtin_amdgcn_sched_barrier(0); } while (0)
#define NOW() ((int)((u32)__builtin_amdgcn_s_memrealtime() - tstart))
template <int DUE, int LATE, int EARLY> __device__ __forceinline__ void pace2(u32 tstart) {
    u64 t;
    __builtin_amdgcn_sched_barrier(0);
    asm volatile("s_memrealtime %0\n\ts_waitcnt vmcnt(%2) lgkmcnt(0)" : "=s"(t) : "n"(0), "n"(LATE - 1) : "memory");
    asm volatile("s_cmpk_gt_i32 %0, %1\n\ts_cbranch_scc1 1f\n\ts_waitcnt vmcnt(%2)\n1:" :: "s"((u32)t - tstart), "n"(DUE), "n"(EARLY - 1) : "scc", "memory");
    __builtin_amdgcn_sched_barrier(0);
}
#define PACE2(due, late, early) pace2<due, late, early>(tstart)
#define KPACE(j) PACE2(T0 + (j) * DT, KLATE, KWIN)

template <int I, int N, class F> __device__ __forceinline__ void sfor(F&& f) { if constexpr (I < N) { f(std::integral_constant<int, I>{}); sfor<I + 1, N>(f); } }

__device__ __forceinline__ float u2f(u32 u) { return __builtin_bit_cast(float, u); }
__device__ __forceinline__ u32 f2u(float f) { return __builtin_bit_cast(u32, f); }
__device__ __forceinline__ float blo(u32 u) { return u2f(u << 16); }
__device__ __forceinline__ float bhi(u32 u) { return u2f(u & 0xFFFF0000u); }
__device__ __forceinline__ u32 pk(float a, float b) { v2bf r = {(__bf16)a, (__bf16)b}; return __builtin_bit_cast(u32, r); }
__device__ __forceinline__ float rbf(float a) { return (float)(__bf16)a; }
template <int CTRL> __device__ __forceinline__ float dpp(float x) { return u2f(__builtin_amdgcn_mov_dpp(f2u(x), CTRL, 0xf, 0xf, true)); }
constexpr int XOR1 = 0xB1, XOR2 = 0x4E, XOR7 = 0x141, ROR8 = 0x128, ROR4 = 0x124;
__device__ __forceinline__ float tr32(float a, float b) { auto s = __builtin_amdgcn_permlane32_swap(f2u(a), f2u(b), false, false); return u2f(s[0]) + u2f(s[1]); }
__device__ __forceinline__ float tr16(float a, float b) { auto s = __builtin_amdgcn_permlane16_swap(f2u(a), f2u(b), false, false); return u2f(s[0]) + u2f(s[1]); }
__device__ __forceinline__ float tr8(float a, float b, bool bit) { const float own = bit ? b : a, oth = bit ? a : b; return own + dpp<ROR8>(oth); }
__device__ __forceinline__ float fmx(float a, float b) { return __builtin_amdgcn_fmed3f(a, b, __builtin_inff()); }
__device__ __forceinline__ float max32(float a) { auto s = __builtin_amdgcn_permlane32_swap(f2u(a), f2u(a), false, false); return fmx(u2f(s[0]), u2f(s[1])); }
__device__ __forceinline__ float max16(float a) { auto s = __builtin_amdgcn_permlane16_swap(f2u(a), f2u(a), false, false); return fmx(u2f(s[0]), u2f(s[1])); }
__device__ __forceinline__ float sum8lo(float a) { a += dpp<XOR1>(a); a += dpp<XOR2>(a); a += dpp<XOR7>(a); return a; }
__device__ __forceinline__ float sum32lo(float a) { a = sum8lo(a); a += dpp<ROR8>(a); return tr16(a, a); }
__device__ __forceinline__ float sum64(float a) { a = sum32lo(a); return tr32(a, a); }
__device__ __forceinline__ v4f mfma(v4u a, v4u b, v4f c) { return __builtin_amdgcn_mfma_f32_16x16x32_bf16(__builtin_bit_cast(v8bf, a), __builtin_bit_cast(v8bf, b), c, 0, 0, 0); }
__device__ __forceinline__ u32 plo(u32 r0, u32 r1) { return __builtin_amdgcn_perm(r1, r0, 0x05040100u); }
__device__ __forceinline__ u32 phi(u32 r0, u32 r1) { return __builtin_amdgcn_perm(r1, r0, 0x07060302u); }
template <int DD> __device__ __forceinline__ v4u col_frag(const v4u* r) {
    constexpr int w = DD >> 1;
    if constexpr (DD & 1) return v4u{phi(r[0][w], r[1][w]), phi(r[2][w], r[3][w]), phi(r[4][w], r[5][w]), phi(r[6][w], r[7][w])};
    else return v4u{plo(r[0][w], r[1][w]), plo(r[2][w], r[3][w]), plo(r[4][w], r[5][w]), plo(r[6][w], r[7][w])};
}
__device__ __forceinline__ void all_frags(const v4u* r, v4u* f) {
#pragma unroll
    for (int k = 0; k < 4; ++k) {
#pragma unroll
        for (int w = 0; w < 4; ++w) { f[2 * w][k] = plo(r[2 * k][w], r[2 * k + 1][w]); f[2 * w + 1][k] = phi(r[2 * k][w], r[2 * k + 1][w]); }
        __builtin_amdgcn_sched_barrier(0);
    }
}
__device__ __forceinline__ void rows_mfma(v4u a, const v4u* r, v4f* acc) {
    acc[0] = mfma(a, col_frag<0>(r), acc[0]); acc[1] = mfma(a, col_frag<1>(r), acc[1]);
    acc[2] = mfma(a, col_frag<2>(r), acc[2]); acc[3] = mfma(a, col_frag<3>(r), acc[3]);
    acc[4] = mfma(a, col_frag<4>(r), acc[4]); acc[5] = mfma(a, col_frag<5>(r), acc[5]);
    acc[6] = mfma(a, col_frag<6>(r), acc[6]); acc[7] = mfma(a, col_frag<7>(r), acc[7]);
}
__device__ __forceinline__ u64 mix64(u64 z) { z += 0x9E3779B97F4A7C15ull; z = (z ^ (z >> 30)) * 0xBF58476D1CE4E5B9ull; z = (z ^ (z >> 27)) * 0x94D049BB133111EBull; return z ^ (z >> 31); }

__device__ __forceinline__ u32 lds_word(LAS u32* ctrl, int idx) { return (u32)__builtin_amdgcn_readfirstlane(*(volatile LAS u32*)(ctrl + idx)); }
template <int SLEEP> __device__ __forceinline__ void lds_wait(LAS u32* ctrl, int idx, u32 want) {
#pragma unroll 1
    for (u32 s = 0; s < LDS_SPIN_CAP; ++s) {
        if (lds_word(ctrl, idx) >= want) break;
        if (SLEEP) __builtin_amdgcn_s_sleep(SLEEP);
    }
    FENCE();
}
template <int IDX> __device__ __forceinline__ void lds_arrive_at(u32 base) {
    asm volatile("s_mov_b64 exec, 1\n\tds_add_u32 %0, %1 offset:%2\n\ts_mov_b64 exec, -1" :: "v"(base), "v"(1u), "n"(IDX * 4) : "memory");
    __builtin_amdgcn_sched_barrier(0);
}
#define lds_arrive(ctrl, idx, lane) lds_arrive_at<idx>((u32)(size_t)(ctrl))
#define lds_arrive2(ctrl, idx, sel) lds_arrive_at<idx>((u32)(size_t)((ctrl) + (sel)))

__device__ __forceinline__ void layernorm16(u32 (&x)[8]) {
    v2f v[8], s = {0.f, 0.f}, q = {0.f, 0.f};
#pragma unroll
    for (int i = 0; i < 8; ++i) { v[i] = v2f{blo(x[i]), bhi(x[i])}; s += v[i]; }
    const float mean = sum64(s.x + s.y) * (1.0f / D);
#pragma unroll
    for (int i = 0; i < 8; ++i) { const v2f d = v[i] - mean; q += d * d; }
    const float var = rbf(sum64(q.x + q.y) * (1.0f / D)), m = rbf(mean);
    const float inv = __builtin_amdgcn_rcpf(rbf(__builtin_amdgcn_sqrtf(rbf(var + 1e-5f))));
#pragma unroll
    for (int i = 0; i < 8; ++i) { const v2f d = v[i] - m; const u32 db = pk(d.x, d.y); const v2f o = v2f{blo(db), bhi(db)} * inv; x[i] = pk(o.x, o.y); }
}

template <int N, int SLEEP> __device__ __forceinline__ void poll(v4u (&a)[N], rsrc_t ws, const int (&voff)[N], const int (&soff)[N], u32 tl, u32 th) {
#pragma unroll 1
    for (u32 s = 0; s < G_SPIN_CAP; ++s) {
        u32 bad = 0;
#pragma unroll
        for (int i = 0; i < N; ++i) bad |= (a[i].y ^ tl) | (a[i].w ^ th);
        const bool miss = bad != 0;
        if (!__any(miss)) break;
        if (SLEEP) __builtin_amdgcn_s_sleep(SLEEP);
        FENCE();
        if (miss) {
#pragma unroll
            for (int i = 0; i < N; ++i) a[i] = ld16<AUX_SC1>(ws, voff[i], soff[i]);
        }
    }
    FENCE();
}
__device__ __forceinline__ void lds_wait_hot(LAS u32* ctrl, int idx, u32 want) {
    u32 t, n;
    asm volatile("s_mov_b32 %1, %4\n1:\n\tds_read_b32 %0, %2\n\ts_sub_u32 %1, %1, 1\n\ts_waitcnt lgkmcnt(0)\n\tv_cmp_gt_u32 vcc, %3, %0\n\ts_cbranch_scc1 2f\n\ts_cbranch_vccnz 1b\n2:"
                 : "=&v"(t), "=&s"(n) : "v"((u32)(size_t)(ctrl + idx)), "s"(want), "n"(LDS_SPIN_CAP) : "vcc", "scc", "memory");
    __builtin_amdgcn_sched_barrier(0);
}
}

__global__ void __launch_bounds__(NTHR, 3) fused_decode(Params) {
    __shared__ __attribute__((aligned(16))) unsigned char smem[L_END];
    LAS unsigned char* L = (LAS unsigned char*)smem;
    LAS u32* ctrl = (LAS u32*)(L + L_CTRL);
    typedef u64 v8q __attribute__((ext_vector_type(8)));
    typedef u64 v2q __attribute__((ext_vector_type(2)));
    v8q ka; v2q kb;
    u64 kc, pc0, tkv = 0;
    asm volatile("s_load_dwordx16 %0, %4, 0x0\n\ts_load_dwordx4 %1, %4, 0x40\n\ts_load_dwordx2 %2, %4, 0x50\n\ts_getpc_b64 %3"
                 : "=&s"(ka), "=&s"(kb), "=&s"(kc), "=&s"(pc0) : "s"(__builtin_amdgcn_kernarg_segment_ptr()) : "memory");
    const int c = blockIdx.x, lane = threadIdx.x & 63, wave = __builtin_amdgcn_readfirstlane(threadIdx.x >> 6);
    const u32 cpf = ld4<0>(mk_rsrc((const void*)(pc0 & ~255ull), wave >= 8 ? CODE_PREFETCH : 0), lane * 128 + (wave & 3) * 8192, 0);
    ctrl[min(lane, 47)] = lane == L_ONE / 4 + 8 ? 0x3F800000u : 0u;
    const int ww = wave & 3, mat = ww & 1, half = ww >> 1, kq = c >> 6, r0 = 256 * kq + 64 * ww;
    int woff = ((r0 + (lane >> 3)) << 13) + (c & 63) * 128 + (lane & 7) * 16, xoff = wave * D * 2 + lane * 32;
    asm volatile("" : "+v"(woff), "+v"(xoff));
    asm volatile("s_waitcnt lgkmcnt(0)\n\ts_cmp_lt_u32 %4, 4\n\ts_cbranch_scc1 1f\n\ts_barrier\n\ts_memrealtime %3\n\ts_waitcnt lgkmcnt(0)\n1:"
                 : "+s"(ka), "+s"(kb), "+s"(kc), "+s"(tkv) : "s"(wave) : "scc", "memory");
    __builtin_amdgcn_sched_barrier(0);
    u32 tstart, tbar;
    Params p;
    p.x = (const u16*)ka[0]; p.wq = (const u16*)ka[1]; p.w1 = (const u16*)ka[2]; p.ws = (unsigned char*)ka[3];
    p.k = (const u16*)ka[4]; p.v = (const u16*)ka[5]; p.wo = (const u16*)ka[6]; p.w2 = (const u16*)ka[7];
    p.b1 = (const u16*)kb[0]; p.b2 = (const u16*)kb[1]; p.out = (u16*)kc;
    const rsrc_t ws = mk_rsrc(p.ws, WS_END);
#define INDICES() const int g = lane >> 4, c16 = lane & 15, grp = c >> 5, rb = (c & 7) + 8 * (c >> 5), cb4 = (c >> 3) & 3
    if (__builtin_expect(wave < 4, 1)) {
        const rsrc_t rx = mk_rsrc(p.x, 2 * D * 2), rw = mk_rsrc(p.wq, D * NQ * 2);
        const v4u x0 = ld16<0>(rx, xoff, 0), x1 = ld16<0>(rx, xoff, 16);
        v2u ep = ld8<AUX_SC1>(ws, wave == 0 ? 0 : WS_OOB, WS_EPOCH);
        v4u R1[8];
#pragma unroll
        for (int i = 0; i < 8; ++i) R1[i] = ld16<AUX_NT>(rw, woff, i * 8 * NQ * 2);
        __builtin_amdgcn_s_setprio(2);
        FENCE();
        asm volatile("s_barrier" ::: "memory");
        FENCE();
        tstart = tbar = (u32)__builtin_amdgcn_s_memrealtime();
        INDICES();
        if (ww < 2) {
            asm volatile("" : "+v"(ep));
            const u64 tag = mix64(((u64)(u32)__builtin_amdgcn_readfirstlane(ep.y) << 32) | (u32)__builtin_amdgcn_readfirstlane(ep.x));
            if (ww == 0 && lane == 0) { ctrl[C_TAG] = (u32)tag; ctrl[C_TAG + 1] = (u32)(tag >> 32); }
            st16(v4u{tstart, (u32)tag, tstart, (u32)(tag >> 32)}, ws, ww == 0 && c < 8 && lane < 8 ? WS_T0 + c * 128 + lane * 16 : WS_OOB, 0);
            u32 xv[8] = {x0.x, x0.y, x0.z, x0.w, x1.x, x1.y, x1.z, x1.w};
#pragma unroll 1
            for (int pass = 0; pass < 2; ++pass) {
                layernorm16(xv);
                LAS unsigned char* dst = L + L_XN + pass * 4096 + ww * 2048 + (lane >> 2) * 128 + (lane & 3) * 4;
#pragma unroll
                for (int i = 0; i < 4; ++i) { *(LAS u32*)(dst + i * 16) = plo(xv[i], xv[i + 4]); *(LAS u32*)(dst + 64 + i * 16) = phi(xv[i], xv[i + 4]); }
                lds_arrive2(ctrl, C_XN, pass);
            }
        }
        lds_wait<1>(ctrl, C_XN, 2);
        const u32 tl = ctrl[C_TAG], th = ctrl[C_TAG + 1];
#define TILE_GEMV(ACT, CNT, OWNER, ...) do { \
            const v4u a0 = *(const LAS v4u*)(L + (ACT) + ((c16 >> 2) & 1) * 2048 + r0 * 2 + (lane & 1) * 64 + g * 16); \
            v4f ac[8]; \
            _Pragma("unroll") for (int e = 0; e < 8; ++e) ac[e] = v4f{0.f, 0.f, 0.f, 0.f}; \
            rows_mfma(a0, R1, ac); \
            float t[4]; \
            _Pragma("unroll") for (int e = 0; e < 4; ++e) { \
                const float l0 = ac[e][0], l1 = ac[e][1], h0 = ac[4 + e][0], h1 = ac[4 + e][1]; \
                const float t0 = (g & 2) ? h0 : l0, t1 = (g & 2) ? h1 : l1; \
                t[e] = (lane & 8) ? t1 : t0; \
            } \
            const v2f part = {tr8(t[0], t[2], lane & 8), tr8(t[1], t[3], lane & 8)}; \
            LAS v2f* xch = (LAS v2f*)(L + L_X2 + lane * 8); \
            xch[ww * 64] = part; \
            lds_arrive(ctrl, CNT, lane); \
            if (ww == (OWNER)) { \
                lds_wait_hot(ctrl, CNT, 4); \
                const v2f u = (xch[0] + xch[64]) + (xch[128] + xch[192]); \
                const int col = 8 * (lane & 7) + 4 * (g >> 1) + 2 * ((lane >> 3) & 1), b = g & 1; \
                __VA_ARGS__ \
            } } while (0)
        TILE_GEMV(L_XN, C_PQ, 2, {
            const u32 own = pk(u.x, u.y), nxt = __builtin_amdgcn_mov_dpp(own, ROR8, 0xf, 0xf, true);
            st16(v4u{own, tl, nxt, th}, ws, (lane & 8) ? WS_OOB : WS_QP + ((kq * 2 + b) * NQ + (c & 63) * 64 + col) * 4, 0);
        });
        if (ww == 3) {
            while (NOW() < TREAD) __builtin_amdgcn_s_sleep(1);
            FENCE();
            const v4u r = ld16<AUX_SC1>(ws, WS_T0 + (lane & 7) * 128, 0);
            const int d = (int)(r.x - tstart);
            const float rel = r.y == tl && r.w == th && d > -1000 && d < 1000 ? (float)d : (float)(TREAD - 150);
            float mx = fmx(rel, dpp<XOR1>(rel)); mx = fmx(mx, dpp<XOR2>(mx)); mx = fmx(mx, dpp<XOR7>(mx));
            ctrl[C_BASE] = (u32)(int)(sum8lo(rel) * 0.125f);
            ctrl[C_BASE + 1] = (u32)(int)mx;
            lds_arrive(ctrl, C_BASE + 2, lane);
        } else lds_wait<2>(ctrl, C_BASE + 2, 1);
        {
            const int due = TQ + (int)lds_word(ctrl, C_BASE + 1);
            while (NOW() < due) __builtin_amdgcn_s_sleep(2);
            tstart += lds_word(ctrl, C_BASE);
        }
        FENCE();
        {
            const int qo = WS_QP + ((grp >> 2) * NQ + (grp & 3) * 1024 + ww * 256) * 4 + lane * 16;
            const int qv[4] = {qo, qo, qo, qo};
            constexpr int qs[4] = {0, 2 * NQ * 4, 4 * NQ * 4, 6 * NQ * 4};
            v4u qp[4];
#pragma unroll
            for (int i = 0; i < 4; ++i) qp[i] = ld16<AUX_SC1>(ws, qv[i], qs[i]);
            FENCE();
            poll<4, 16>(qp, ws, qv, qs, tl, th);
            *(LAS v2u*)(L + L_Q + (2 * ww + (lane >> 5)) * QROW + (lane & 31) * 8) =
                v2u{pk((blo(qp[0].x) + blo(qp[1].x)) + (blo(qp[2].x) + blo(qp[3].x)), (bhi(qp[0].x) + bhi(qp[1].x)) + (bhi(qp[2].x) + bhi(qp[3].x))),
                    pk((blo(qp[0].z) + blo(qp[1].z)) + (blo(qp[2].z) + blo(qp[3].z)), (bhi(qp[0].z) + bhi(qp[1].z)) + (bhi(qp[2].z) + bhi(qp[3].z)))};
            lds_arrive(ctrl, C_Q, lane);
        }
        const int fo = (c & 7) * 32 + (c >> 3), ob = fo >> 7;
        const int n = (fo & 127) * 8 + 4 * (lane & 1) + 2 * (lane >> 5) + (g & 1);
        u32 xb = (u32)((const GAS u16*)p.x)[ob * D + n] | ((u32)((const GAS u16*)p.b2)[n] << 16);
        int po = (half ? WS_PB : WS_PG) + ((rb * 2 + (g & 1)) * 1024 + cb4 * 256 + mat * 128 + c16 * 8 + (g >> 1) * 4) * 4;
        int fin = WS_PB + (((lane >> 1) * 2 + ob) * 1024 + (fo & 127) * 8) * 4 + (lane & 1) * 16;
        asm volatile("" : "+v"(xb), "+v"(po), "+v"(fin));
        lds_wait<2>(ctrl, C_VISS, 8);
        tstart += lds_word(ctrl, C_LATE) >> 1;
        v4u R[16];
        {
            const int cb1 = (c + 4) & 63;
            const rsrc_t r1 = mk_rsrc(p.w1, cb1 < 56 ? D * DFF * 2 : 0);
            const int o1 = (r0 + (lane >> 3)) * (DFF * 2) + cb1 * 128 + (lane & 7) * 16;
            sfor<0, 8>([&](auto I) { constexpr int i = I; R1[i] = ld16<AUX_NT>(r1, o1, i * 8 * DFF * 2); PACE2(T2 + i * DT2, WLATE, WWIN); });
            const rsrc_t r2 = mk_rsrc(half ? p.wo : p.w2, half ? NQ * D * 2 : (56 * rb + 56) * 2048);
            const int o2 = ((half ? 64 : 56) * rb + g) * 2048 + cb4 * 512 + mat * 256 + c16 * 16;
            sfor<0, HPOS>([&](auto I) { constexpr int i = I; R[i] = ld16<AUX_NT>(r2, o2, i * 8192); if constexpr (i < HPOS - 1) PACE2(T2 + (8 + i) * DT2, WLATE, WWIN); });
            FENCE();
            TILE_GEMV(L_Y, C_PH, 3, {
                st16(v4u{f2u(u.x), tl, f2u(u.y), th}, ws, cb1 < 56 ? WS_HP + ((kq * 2 + b) * DFF + cb1 * 64 + col) * 8 : WS_OOB, 0);
            });
            FENCE();
            sfor<HPOS, 16>([&](auto I) { constexpr int i = I; PACE2(T2 + (7 + i) * DT2, WLATE, WWIN); R[i] = ld16<AUX_NT>(r2, o2, i * 8192); });
        }
        FENCE();
        v4u T[8];
        all_frags(R, T);
#pragma unroll
        for (int i = 0; i < 8; ++i) asm volatile("" : "+v"(T[i]));
        u32 ap = half ? L_O + c16 * OROW + g * 16 : (c16 & 11) ? L_ZERO : L_H + ((c16 >> 2) & 1) * 128 + g * 16;
        u32 mp = half ? L_OM + (g & 1) * 16 : L_ONE;
        asm volatile("" : "+v"(ap), "+v"(mp));
        lds_wait_hot(ctrl, half ? C_O : C_H, half ? 8 : 1);
        {
            const v4u a0 = *(const LAS v4u*)(L + ap), a1 = *(const LAS v4u*)(L + ap + 64);
            const v4f om = *(const LAS v4f*)(L + mp), ol = *(const LAS v4f*)(L + mp + 32);
            v4f ac[8];
#pragma unroll
            for (int e = 0; e < 8; ++e) ac[e] = mfma(a0, T[e], v4f{0.f, 0.f, 0.f, 0.f});
            const float M = fmx(fmx(om[0], om[1]), fmx(om[2], om[3]));
            v2f f01 = {__builtin_amdgcn_exp2f(om[0] - M), __builtin_amdgcn_exp2f(om[1] - M)}, f23 = {__builtin_amdgcn_exp2f(om[2] - M), __builtin_amdgcn_exp2f(om[3] - M)};
            const v2f dn = f01 * v2f{ol[0], ol[1]} + f23 * v2f{ol[2], ol[3]};
            const float inv = __builtin_amdgcn_rcpf(dn.x + dn.y);
            f01 *= inv; f23 *= inv;
            __builtin_amdgcn_sched_barrier(0);
            v4u U[8];
            all_frags(R + 8, U);
#pragma unroll
            for (int e = 0; e < 8; ++e) ac[(e >> 1) + 4 * (e & 1)] = mfma(a1, U[(e >> 1) + 4 * (e & 1)], ac[(e >> 1) + 4 * (e & 1)]);
            __builtin_amdgcn_sched_barrier(0);
            float t[8];
#pragma unroll
            for (int e = 0; e < 8; ++e) { const v2f r = v2f{ac[e][0], ac[e][1]} * f01 + v2f{ac[e][2], ac[e][3]} * f23; t[e] = r.x + r.y; }
            st16(v4u{pk(tr32(t[0], t[4]), tr32(t[1], t[5])), tl, pk(tr32(t[2], t[6]), tr32(t[3], t[7])), th}, ws, po, 0);
        }
        if (half) lds_arrive(ctrl, C_PB, lane);
        if (ww == 0) {
            constexpr int RS = 32 * 2 * 1024 * 4, GO = WS_PG - WS_PB;
            const int fv[4] = {fin, fin, fin, fin};
            constexpr int fs[4] = {0, RS, GO, GO + RS};
            lds_wait_hot(ctrl, C_PB, 2);
            const int now = NOW(), due = min(max(TBT, now + BMIN), now + BMAX);
            while (NOW() < due) {}
            FENCE();
            v4u pb[4] = {ld16<AUX_SC1>(ws, fin, fs[0]), ld16<AUX_SC1>(ws, fin, fs[1]), ld16<AUX_SC1>(ws, fin, fs[2]), ld16<AUX_SC1>(ws, fin, fs[3])};
            FENCE();
            poll<4, 0>(pb, ws, fv, fs, tl, th);
            float s = tr16(tr32(blo(pb[0].x) + blo(pb[1].x), blo(pb[0].z) + blo(pb[1].z)), tr32(bhi(pb[0].x) + bhi(pb[1].x), bhi(pb[0].z) + bhi(pb[1].z)));
            float u = tr16(tr32(blo(pb[2].x) + blo(pb[3].x), blo(pb[2].z) + blo(pb[3].z)), tr32(bhi(pb[2].x) + bhi(pb[3].x), bhi(pb[2].z) + bhi(pb[3].z)));
            s += dpp<ROR8>(s); u += dpp<ROR8>(u);
            s += dpp<ROR4>(s); u += dpp<ROR4>(u);
            s += dpp<XOR2>(s); u += dpp<XOR2>(u);
            u = rbf(rbf(u) + bhi(xb));
            const float gate = rbf(u * __builtin_amdgcn_rcpf(1.0f + __builtin_amdgcn_exp2f(-1.4426950408889634f * u)));
            if ((lane & 14) == 0) ((GAS u16*)p.out)[ob * D + n] = (u16)pk(blo(xb) + rbf(rbf(s) * gate), 0.f);
            if (c == 0 && lane == 0) st8(v2u{tl, th}, ws, WS_EPOCH, 0);
        }
    } else {
        const int kw = wave - 4;
        tstart = tbar = (u32)tkv;
        INDICES();
        (void)cb4;
        asm volatile("" :: "v"(cpf));
        const rsrc_t rk = mk_rsrc(p.k, 2 * 4 * SK * 128 * 2), rv = mk_rsrc(p.v, 2 * 4 * SK * 128 * 2);
        const int tb = grp * (SK * 256) + ((c & 31) * 8 + kw) * 4096;
        constexpr int TS = 1 << 20;
        v4u K[16], V[16];
        {
            const int ko = tb + (lane & 7) * 256 + (g + 4 * ((lane >> 3) & 1)) * 16;
            sfor<0, 16>([&](auto I) {
                constexpr int i = I;
                K[i] = ld16<AUX_NT>(rk, ko + (i & 1) * 2048 + ((i >> 1) & 1) * 128, (i >> 2) * TS);
                if (i == 7 || i == 10 || i == 14) tstart = tbar + lds_word(ctrl, C_BASE);
                KPACE(i);
            });
        }
        const int vo = tb + g * 1024 + c16 * 16;
#define LDV(j) V[j] = ld16<AUX_NT>(rv, vo + ((j) & 3) * 256, ((j) >> 2) * TS)
        sfor<0, 8>([&](auto J) {
            constexpr int j = J;
            LDV(j);
            FENCE();
            constexpr int pr = 2 * j;
#pragma unroll
            for (int w = 0; w < 4; ++w) {
                const u32 a = K[pr][w], b = K[pr + 1][w];
                K[pr][w] = __builtin_amdgcn_update_dpp(a, b, ROR8, 0xf, 0xc, false);
                K[pr + 1][w] = __builtin_amdgcn_update_dpp(b, a, ROR8, 0xf, 0x3, false);
            }
            v4u& k0 = K[pr]; v4u& k1 = K[pr + 1];
            asm volatile("" : "+v"(k0), "+v"(k1));
            KPACE(16 + j);
        });
        sfor<8, QKPOS>([&](auto J) { constexpr int j = J; LDV(j); if constexpr (j < QKPOS - 3) KPACE(16 + j); });
        FENCE();
        lds_wait<1>(ctrl, C_Q, 4);
        v4f S[4];
        {
            v4u q[4];
#pragma unroll
            for (int cc = 0; cc < 4; ++cc) q[cc] = *(const LAS v4u*)(L + L_Q + (lane & 7) * QROW + cc * 64 + g * 16);
#pragma unroll
            for (int t = 0; t < 4; ++t) {
                S[t] = v4f{0.f, 0.f, 0.f, 0.f};
#pragma unroll
                for (int cc = 0; cc < 4; ++cc) S[t] = mfma(K[4 * t + cc], q[cc], S[t]);
            }
        }
        PACE(KWIN + 2);
        LDV(QKPOS);
        FENCE();
        float m = fmx(S[0][0], S[0][1]);
#pragma unroll
        for (int i = 2; i < 16; i += 2) m = fmx(fmx(m, S[i >> 2][i & 3]), S[i >> 2][(i & 3) + 1]);
        m = max32(max16(m)) * C2;
        float l = 0.f;
#pragma unroll
        for (int i = 0; i < 16; ++i) { const float e = __builtin_amdgcn_exp2f(fmaf(S[i >> 2][i & 3], C2, -m)); S[i >> 2][i & 3] = e; l += e; }
        v4u P0 = {pk(S[0][0], S[0][1]), pk(S[0][2], S[0][3]), pk(S[1][0], S[1][1]), pk(S[1][2], S[1][3])};
        l = tr16(l, l); l = tr32(l, l);
        if (lane < 8) { *(LAS float*)(L + L_M + lane * 32 + kw * 4) = m; *(LAS float*)(L + L_L + lane * 32 + kw * 4) = l; }
        lds_arrive(ctrl, C_MX, lane);
        asm volatile("" : "+v"(P0), "+v"(S[2]), "+v"(S[3]));
        sfor<QKPOS + 1, 16>([&](auto J) { constexpr int j = J; KPACE(15 + j); LDV(j); });
        FENCE();
        asm volatile("s_mov_b64 exec, 1\n\tds_max_i32 %0, %1 offset:%2\n\ts_mov_b64 exec, -1"
                     :: "v"((u32)(size_t)ctrl), "v"(min(NOW() - (T0 + 31 * DT + ASLACK), AMAX)), "n"(C_LATE * 4) : "memory");
        lds_arrive(ctrl, C_VISS, lane);
        v4f acc[8];
#pragma unroll
        for (int e = 0; e < 8; ++e) acc[e] = v4f{0.f, 0.f, 0.f, 0.f};
        rows_mfma(P0, V, acc);
        FENCE();
        lds_wait<1>(ctrl, C_MX, 8);
        const u32 tl = ctrl[C_TAG], th = ctrl[C_TAG + 1];
        const int s0 = ((kw >> 2) * 4 + (rb >> 4)) * 32 + ((8 * kw + g) & 31), hh = (rb >> 1) & 7;
        int pao = lane < 32 ? WS_PA + c * 4096 + kw * 512 + c16 * 32 + (g & 1) * 16 : WS_OOB;
        int ao = WS_PA + s0 * 4096 + hh * 512 + (rb & 1) * 256 + c16 * 16, mo = WS_ML + (s0 + (lane & 8) / 2) * 128 + hh * 16;
        u32 rdo = L_ACC + 4 * (lane >> 5) * 4096 + kw * 512 + (lane & 31) * 16, oo = L_O + kw * OROW + (c16 >> 3) * 64 + g * 16 + (c16 & 7) * 2;
        u32 wro = L_ACC + kw * 4096 + 4 * (g & 1) * 512 + (g >> 1) * 256 + c16 * 16;
        asm volatile("" : "+v"(rdo), "+v"(oo), "+v"(wro));
        v4u P1;
        v2u mlv;
        {
            const v4f m0 = *(const LAS v4f*)(L + L_M + (lane & 7) * 32), m1 = *(const LAS v4f*)(L + L_M + (lane & 7) * 32 + 16);
            const float M = fmaxf(fmaxf(fmaxf(m0[0], m0[1]), fmaxf(m0[2], m0[3])), fmaxf(fmaxf(m1[0], m1[1]), fmaxf(m1[2], m1[3])));
            const float f = __builtin_amdgcn_exp2f(m - M);
            v4f rs;
#pragma unroll
            for (int r = 0; r < 4; ++r) rs[r] = u2f(__builtin_amdgcn_ds_bpermute((4 * (g & 1) + r) * 4, f2u(f)));
#pragma unroll
            for (int e = 0; e < 8; ++e) acc[e] *= rs;
            P1 = v4u{pk(S[2][0] * f, S[2][1] * f), pk(S[2][2] * f, S[2][3] * f), pk(S[3][0] * f, S[3][1] * f), pk(S[3][2] * f, S[3][3] * f)};
            const v4f l0 = *(const LAS v4f*)(L + L_L + (lane & 7) * 32), l1 = *(const LAS v4f*)(L + L_L + (lane & 7) * 32 + 16);
            float lt = 0.f;
#pragma unroll
            for (int w = 0; w < 4; ++w) lt += l0[w] * __builtin_amdgcn_exp2f(m0[w] - M) + l1[w] * __builtin_amdgcn_exp2f(m1[w] - M);
            mlv = v2u{f2u(M), f2u(lt)};
            asm volatile("" : "+v"(pao), "+v"(ao), "+v"(mo), "+v"(P1), "+v"(mlv), "+v"(acc[0]), "+v"(acc[1]), "+v"(acc[2]), "+v"(acc[3]), "+v"(acc[4]), "+v"(acc[5]), "+v"(acc[6]), "+v"(acc[7]));
        }
        FENCE();
        {
            v4u F[8];
            all_frags(V + 8, F);
#pragma unroll
            for (int e = 0; e < 8; ++e) acc[(e >> 1) + 4 * (e & 1)] = mfma(P1, F[(e >> 1) + 4 * (e & 1)], acc[(e >> 1) + 4 * (e & 1)]);
            __builtin_amdgcn_sched_barrier(0);
        }
#pragma unroll
        for (int r = 0; r < 4; ++r) {
            v4f t;
#pragma unroll
            for (int e = 0; e < 4; ++e) { const float lo = acc[e][r], hi = acc[4 + e][r]; t[e] = (g & 2) ? hi : lo; }
            *(LAS v4f*)(L + wro + r * 512) = t;
        }
        lds_arrive(ctrl, C_ACC, lane);
        lds_wait_hot(ctrl, C_ACC, 8);
        {
            const v4f t = (*(const LAS v4f*)(L + rdo) + *(const LAS v4f*)(L + rdo + 4096)) + (*(const LAS v4f*)(L + rdo + 8192) + *(const LAS v4f*)(L + rdo + 12288));
            st16(v4u{pk(tr32(t[0], t[0]), tr32(t[1], t[1])), tl, pk(tr32(t[2], t[2]), tr32(t[3], t[3])), th}, ws, pao, 0);
            if (kw == 7) st16(v4u{mlv.x, tl, mlv.y, th}, ws, lane < 8 ? WS_ML + c * 128 + lane * 16 : WS_OOB, 0);
        }
        const int gi = lane & 31, col = 56 * rb + 2 * (gi < 28 ? gi : 0), ho = WS_HP + ((lane >> 5) * DFF + col) * 8;
        const int hv[4] = {ho, ho, ho, ho};
        constexpr int hs[4] = {0, 2 * DFF * 8, 4 * DFF * 8, 6 * DFF * 8};
        const u32 bias = ld4<0>(mk_rsrc(p.b1, kw == 0 ? DFF * 2 : 0), col * 2, 0);
        v4u hp[4];
        {
            const int av[3] = {ao, ao, mo};
            constexpr int as[3] = {0, 4 * 4096, 0};
            const int due = TA + (int)lds_word(ctrl, C_LATE);
            while (NOW() < due) {}
            FENCE();
            v4u a[3] = {ld16<AUX_SC1>(ws, ao, as[0]), ld16<AUX_SC1>(ws, ao, as[1]), ld16<AUX_SC1>(ws, mo, as[2])};
            FENCE();
            poll<3, 0>(a, ws, av, as, tl, th);
            if (kw == 0) {
                while (NOW() < due + (TH - TA)) {}
                FENCE();
#pragma unroll
                for (int i = 0; i < 4; ++i) hp[i] = ld16<AUX_SC1>(ws, hv[i], hs[i]);
                FENCE();
            }
            const float mo_ = u2f(a[2].x), mw = max32(max16(fmx(mo_, dpp<ROR8>(mo_))));
            const float sc = __builtin_amdgcn_exp2f(mo_ - mw), so_ = dpp<ROR8>(sc);
            const float sc0 = (lane & 8) ? so_ : sc, sc1 = (lane & 8) ? sc : so_;
            const float t0 = fmaf(sc0, blo(a[0].x), sc1 * blo(a[1].x)), t1 = fmaf(sc0, bhi(a[0].x), sc1 * bhi(a[1].x));
            const float t2 = fmaf(sc0, blo(a[0].z), sc1 * blo(a[1].z)), t3 = fmaf(sc0, bhi(a[0].z), sc1 * bhi(a[1].z));
            float lw = sc * u2f(a[2].z);
            lw += dpp<ROR8>(lw);
            const float o = tr16(tr32(t0, t2), tr32(t1, t3));
            lw = tr16(lw, lw); lw = tr32(lw, lw);
            const float hi = rbf(o);
            *(LAS u16*)(L + oo) = (u16)pk(hi, 0.f);
            *(LAS u16*)(L + oo + 8 * OROW) = (u16)pk(o - hi, 0.f);
            *(LAS float*)(L + L_OM + kw * 4) = mw; *(LAS float*)(L + L_OM + 32 + kw * 4) = lw;
            lds_arrive(ctrl, C_O, lane);
        }
        if (kw == 0) {
            poll<4, 2>(hp, ws, hv, hs, tl, th);
#pragma unroll
            for (int e = 0; e < 2; ++e) {
                const float s = (u2f(hp[0][2 * e]) + u2f(hp[1][2 * e])) + (u2f(hp[2][2 * e]) + u2f(hp[3][2 * e]));
                const float u = rbf(rbf(s) + (e ? bhi(bias) : blo(bias)));
                const float z2 = 2.0f * 0.7978845608028654f * 1.4426950408889634f * fmaf(0.044715f * u * u, u, u);
                const float h = u * __builtin_amdgcn_rcpf(1.0f + __builtin_amdgcn_exp2f(-z2));
                *(LAS u16*)(L + L_H + (((lane >> 5) * 2 + (gi >> 4)) * 4 + 2 * (gi & 1) + e) * 16 + ((gi >> 1) & 7) * 2) = gi < 28 ? (u16)pk(h, 0.f) : (u16)0;
            }
            lds_arrive(ctrl, C_H, lane);
        }
    }
}

extern "C" void kernel_launch(void* const* d_in, const int* in_sizes, int n_in, void* d_out, int out_size, void* d_ws, size_t ws_size,
                              hipStream_t stream) {
    Params p;
    p.x = (const u16*)d_in[0]; p.k = (const u16*)d_in[1]; p.v = (const u16*)d_in[2]; p.wq = (const u16*)d_in[3]; p.wo = (const u16*)d_in[4];
    p.w1 = (const u16*)d_in[6]; p.b1 = (const u16*)d_in[7]; p.w2 = (const u16*)d_in[8]; p.b2 = (const u16*)d_in[9];
    p.out = (u16*)d_out; p.ws = (unsigned char*)d_ws;
    fused_decode<<<256, NTHR, 0, stream>>>(p);
}
```
